```python
import torch

def _softmax(x, axis=-1):
    return torch.softmax(x, dim=axis)

def _gelu(x):
    return torch.nn.functional.gelu(x, approximate='tanh')

def _silu(x):
    return torch.nn.functional.silu(x)

def _sigmoid(x):
    return torch.sigmoid(x)

def _layernorm(x, axis=-1, eps=1e-05):
    m = x.mean(dim=axis, keepdim=True)
    v = x.var(dim=axis, keepdim=True, unbiased=False)
    return (x - m) / torch.sqrt(v + eps)

def _scatter_add(tgt, idx, src, axis=0):
    return tgt.clone().index_add_(axis, idx, src)

def _segment_sum(src, seg, n):
    out = torch.zeros((n,) + src.shape[1:], dtype=src.dtype)
    return out.index_add_(0, seg, src)

def _pad(x, pw):
    flat = tuple((v for pair in reversed(pw) for v in pair))
    return torch.nn.functional.pad(x, flat)
kernel_name = 'sequential_preLN__memory_aug_recurrent_mem_tr__glu_gated_reglu__prompt'

def setup_inputs():

    _w = lambda a: (lambda t: t.to(torch.bfloat16) if t.is_floating_point() else t)(torch.as_tensor(a))

    def _A_setup():
        B, S, Dm = (1, 2048, 448)
        H, Dh = (16, 256)
        M, seg = (8, 32)
        seg = min(seg, S)
        S = S // seg * seg
        return {'x': _w((lambda *s: torch.randn(*s, dtype=torch.bfloat16) * 0.05)(B, S, Dm)), 'mem0': _w((lambda *s: torch.randn(*s, dtype=torch.bfloat16) * 0.2)(B, M, Dm)), 'Wq': _w((lambda *s: torch.randn(*s, dtype=torch.bfloat16) * 1.6)(Dm, H, Dh) * Dm ** (-0.5)), 'Wk': _w((lambda *s: torch.randn(*s, dtype=torch.bfloat16) * 1.6)(Dm, H, Dh) * Dm ** (-0.5)), 'Wv': _w((lambda *s: torch.randn(*s, dtype=torch.bfloat16) * 0.2)(Dm, H, Dh) * Dm ** (-0.5)), 'Wo': _w((lambda *s: torch.randn(*s, dtype=torch.bfloat16) * 0.2)(H, Dh, Dm) * (H * Dh) ** (-0.5)), 'seg_len': seg}
    _A_in = _A_setup()

    def _B_setup():
        B, S, D, F = (1, 2048, 448, 1792)
        return {'x': _w((lambda *s: torch.randn(*s, dtype=torch.bfloat16) * 0.05)(B, S, D)), 'W_gate': _w((lambda *s: torch.randn(*s, dtype=torch.bfloat16) * 0.2)(D, F) * (2.0 / D) ** 0.5), 'W_up': _w((lambda *s: torch.randn(*s, dtype=torch.bfloat16) * 0.2)(D, F) * (2.0 / D) ** 0.5), 'W_down': _w((lambda *s: torch.randn(*s, dtype=torch.bfloat16) * 0.2)(F, D) * (1.0 / F) ** 0.5)}
    _B_in = _B_setup()
    return {'A_x': _A_in['x'], 'A_mem0': _A_in['mem0'], 'A_Wq': _A_in['Wq'], 'A_Wk': _A_in['Wk'], 'A_Wv': _A_in['Wv'], 'A_Wo': _A_in['Wo'], 'A_seg_len': _A_in['seg_len'], 'B_x': _B_in['x'], 'B_W_gate': _B_in['W_gate'], 'B_W_up': _B_in['W_up'], 'B_W_down': _B_in['W_down']}

def reference(A_x, A_mem0, A_Wq, A_Wk, A_Wv, A_Wo, A_seg_len, B_x, B_W_gate, B_W_up, B_W_down):

    def _A_ref(x, mem0, Wq, Wk, Wv, Wo, seg_len):
        B, S, Dm = x.shape
        H, Dh = (Wq.shape[1], Wq.shape[2])
        M = mem0.shape[1]
        scale = Dh ** (-0.5)
        n_seg = S // seg_len
        L = M + seg_len
        idx = torch.arange(L)
        mask = (idx[:, None] < M) | (idx[:, None] >= idx[None, :])
        mem = mem0
        outs = []
        for s in range(n_seg):
            xs = x[:, s * seg_len:(s + 1) * seg_len, :].clone()
            h = torch.concatenate([mem, xs], axis=1)
            q = torch.einsum('btd,dhe->bhte', h, Wq)
            k = torch.einsum('btd,dhe->bhte', h, Wk)
            v = torch.einsum('btd,dhe->bhte', h, Wv)
            scores = torch.einsum('bhqe,bhke->bhqk', q, k) * scale
            scores = torch.where(mask[None, None], scores, -1000000000.0)
            p = _softmax(scores, axis=-1)
            o = torch.einsum('bhqk,bhke->bhqe', p, v)
            o = torch.einsum('bhte,hed->btd', o, Wo)
            mem = o[:, :M, :].clone()
            outs.append(o[:, M:, :])
        return torch.concatenate(outs, axis=1)

    def _B_ref(x, W_gate, W_up, W_down):
        gate = torch.einsum('bsd,df->bsf', x, W_gate)
        up = torch.einsum('bsd,df->bsf', x, W_up)
        gate = gate * (lambda x, dt: x.to(dt))(gate > 0, gate.dtype)
        h = gate * up
        return torch.einsum('bsf,fd->bsd', h, W_down)
    x = A_x
    h = x + _A_ref(_layernorm(x, axis=-1), A_mem0, A_Wq, A_Wk, A_Wv, A_Wo, A_seg_len)
    h2 = h + _B_ref(_layernorm(h, axis=-1), B_W_gate, B_W_up, B_W_down)
    return h2
```

```cpp
#include <hip/hip_runtime.h>
#include <hip/hip_bf16.h>

typedef unsigned short u16;
typedef unsigned v4u __attribute__((ext_vector_type(4)));
typedef unsigned v2u __attribute__((ext_vector_type(2)));
typedef float f4 __attribute__((ext_vector_type(4)));
typedef float f2 __attribute__((ext_vector_type(2)));
typedef __bf16 bf8 __attribute__((ext_vector_type(8)));
typedef __bf16 bf2 __attribute__((ext_vector_type(2)));
typedef short s4 __attribute__((ext_vector_type(4)));
typedef __amdgpu_buffer_rsrc_t rsrc_t;

#define DEV __device__ __forceinline__
#define MEMBAR() asm volatile("" ::: "memory")

constexpr unsigned MB = 1u << 20;
constexpr unsigned O_CH = 0;
constexpr unsigned O_XNF = 6 * MB;
constexpr unsigned O_FLG = 7 * MB + 768 * 1024;
constexpr unsigned O_FKV = O_FLG;
constexpr unsigned O_FKM = O_FLG + 16 * 1024;
constexpr unsigned O_FVM = O_FLG + 80 * 1024;
constexpr unsigned O_FP = O_FLG + 144 * 1024;
constexpr unsigned O_FH1 = O_FLG + 148 * 1024;
constexpr unsigned O_FACT = O_FLG + 176 * 1024;
constexpr unsigned O_TICK = O_FLG + 172 * 1024;
constexpr unsigned O_WF = 8 * MB;
constexpr unsigned O_MEM0 = 20 * MB + 512 * 1024;
constexpr unsigned O_CAL = O_MEM0 + 8192;
constexpr unsigned O_FKE = O_MEM0 + 65536;
constexpr unsigned O_KF = 21 * MB;
constexpr unsigned O_VF = 49 * MB;
constexpr unsigned O_KMF = 77 * MB;
constexpr unsigned O_VMF = 84 * MB;
constexpr unsigned O_PF = 91 * MB;
constexpr unsigned O_PM = 93 * MB;
constexpr unsigned O_H1F = 94 * MB;
constexpr unsigned O_ACT = 96 * MB;
constexpr unsigned O_DBG = 104 * MB;
constexpr unsigned WS_SPAN = 108 * MB;
#define TS(s, k)

#define TMD
#define TM(k)
#define TMW(s)
constexpr unsigned SENT = 0xFFFFFFFFu;
constexpr int SC1 = 16;
#define FASTSEG 56
#define FASTH_ 40
#define FASTA_ 58
constexpr int FAST0 = FASTSEG, FASTH = FASTH_, FASTA = FASTA_;
#define NPROBE 4
#define PRE_S 31
#define PRE_M 13
#define PRE_O 0
#define PRE_E 38
#define PRE_P 0

#define DELAY(idx, dflt) do { if ((dflt) > 0) __builtin_amdgcn_s_sleep(dflt); } while (0)
#define HEADW 14
#define HEAD_WAIT() do { for (int i_ = 0; i_ < HEADW; ++i_) __builtin_amdgcn_s_sleep(16); } while (0)
#define FSLEEP __builtin_amdgcn_s_sleep(3)
DEV unsigned pk2(float a, float b) { f2 v = {a, b}; return __builtin_bit_cast(unsigned, __builtin_convertvector(v, bf2)); }
DEV float rbf(float a) { return __uint_as_float(pk2(a, 0.f) << 16); }
DEV float lo16(unsigned u) { return __uint_as_float(u << 16); }
DEV float hi16(unsigned u) { return __uint_as_float(u & 0xFFFF0000u); }
DEV f4 mm32(v4u a, v4u b, f4 c) { return __builtin_amdgcn_mfma_f32_16x16x32_bf16(__builtin_bit_cast(bf8, a), __builtin_bit_cast(bf8, b), c, 0, 0, 0); }
DEV f4 mm16(v2u a, v2u b, f4 c) { return __builtin_amdgcn_mfma_f32_16x16x16bf16_1k(__builtin_bit_cast(s4, a), __builtin_bit_cast(s4, b), c, 0, 0, 0); }
DEV v4u ld4(rsrc_t r, unsigned vo, unsigned so) { return __builtin_amdgcn_raw_buffer_load_b128(r, vo, so, 0); }
DEV v4u ld4c(rsrc_t r, unsigned vo, unsigned so) { return __builtin_amdgcn_raw_buffer_load_b128(r, vo, so, SC1); }
DEV v2u ld2c(rsrc_t r, unsigned vo, unsigned so) { return __builtin_amdgcn_raw_buffer_load_b64(r, vo, so, SC1); }
DEV unsigned ld1c(rsrc_t r, unsigned vo, unsigned so) { return __builtin_amdgcn_raw_buffer_load_b32(r, vo, so, SC1); }
DEV void st4c(v4u v, rsrc_t r, unsigned vo, unsigned so) { __builtin_amdgcn_raw_buffer_store_b128(v, r, vo, so, SC1); }
DEV void st2c(v2u v, rsrc_t r, unsigned vo, unsigned so) { __builtin_amdgcn_raw_buffer_store_b64(v, r, vo, so, SC1); }
DEV void st1c(unsigned v, rsrc_t r, unsigned vo, unsigned so) { __builtin_amdgcn_raw_buffer_store_b32(v, r, vo, so, SC1); }
DEV void vm0() { MEMBAR(); __builtin_amdgcn_s_waitcnt(0x0F70); MEMBAR(); }
DEV unsigned umx(v4u v) { return max(max(v.x, v.y), max(v.z, v.w)); }
DEV unsigned clampS(unsigned x) { return min(x, 0xFFFFFFFEu); }
DEV unsigned pk2c(float a, float b) { return clampS(pk2(a, b)); }
DEV unsigned umx2(v2u v) { return max(v.x, v.y); }
DEV float ex2(float x) { return __builtin_amdgcn_exp2f(x); }
DEV float sw16max(float x) { auto r = __builtin_amdgcn_permlane16_swap(__float_as_uint(x), __float_as_uint(x), false, false); return fmaxf(__uint_as_float(r[0]), __uint_as_float(r[1])); }
DEV float sw32max(float x) { auto r = __builtin_amdgcn_permlane32_swap(__float_as_uint(x), __float_as_uint(x), false, false); return fmaxf(__uint_as_float(r[0]), __uint_as_float(r[1])); }
DEV float sw16add(float x) { auto r = __builtin_amdgcn_permlane16_swap(__float_as_uint(x), __float_as_uint(x), false, false); return __uint_as_float(r[0]) + __uint_as_float(r[1]); }
DEV float sw32add(float x) { auto r = __builtin_amdgcn_permlane32_swap(__float_as_uint(x), __float_as_uint(x), false, false); return __uint_as_float(r[0]) + __uint_as_float(r[1]); }
DEV float ror8(float x) { return __uint_as_float(__builtin_amdgcn_update_dpp(0u, __float_as_uint(x), 0x128, 0xf, 0xf, false)); }
DEV float up8(float x) { return __uint_as_float(__builtin_amdgcn_update_dpp(__float_as_uint(x), __float_as_uint(x), 0x128, 0xf, 0x3, false)); }
DEV v4u gatherT(const u16* W, int N, int krow, int col) {
    unsigned a[8];
#pragma unroll
    for (int e = 0; e < 8; ++e) a[e] = W[(size_t)(krow + e) * N + col];
    v4u r = {a[0] | (a[1] << 16), a[2] | (a[3] << 16), a[4] | (a[5] << 16), a[6] | (a[7] << 16)};
    return r;
}
DEV void wait_flags(rsrc_t rs, unsigned vo, bool active, int& budget) {
    for (;;) {
        MEMBAR();
        unsigned v = 1;
        if (active) v = ld1c(rs, vo, 0);
        if (__all(v != 0)) break;
        if (--budget < 0) break;
        __builtin_amdgcn_s_sleep(6);
    }
    MEMBAR();
}
DEV unsigned page_addr(unsigned pgw, int slot, int q) {
    const int reg = slot / 21, per = 3 * (slot - 21 * reg) + (q >> 2);
    const unsigned pg = (pgw >> (3 * (q & 3))) & 7u;
    return O_CH + (unsigned)reg * (2 * MB) + (unsigned)per * 32768u + pg * 4096u;
}
DEV unsigned mem_addr(unsigned pgw, int slot, int u) {
    return page_addr(pgw, slot, 8 + (u >> 2)) + (unsigned)(u & 3) * 1024u;
}
#define NEARLY 6
DEV unsigned fkv(int s, int k) { return s < NEARLY ? O_FKE + (unsigned)(s * 64 + k) * 128u : O_FKV + (unsigned)(s * 64 + k) * 4u; }
DEV unsigned ror8u(unsigned x) { return __builtin_amdgcn_update_dpp(0u, x, 0x128, 0xf, 0xf, false); }
DEV void fix_pair(v4u& P, v4u& Q) {
#pragma unroll
    for (int i = 0; i < 4; ++i) {
        const unsigned f0 = __builtin_amdgcn_update_dpp(P[i], Q[i], 0x128, 0xf, 0xc, false), f1 = __builtin_amdgcn_update_dpp(Q[i], P[i], 0x128, 0xf, 0x3, false);
        P[i] = f0; Q[i] = f1;
    }
}
DEV void bar_lds() { asm volatile("s_waitcnt lgkmcnt(0)\n\ts_barrier" ::: "memory"); }

__global__ void __launch_bounds__(512, 2) k_prep(const u16* __restrict__ x, const u16* __restrict__ mem0, const u16* __restrict__ Wq,
                                                 const u16* __restrict__ Wk, const u16* __restrict__ Wv, const u16* __restrict__ Wo,
                                                 unsigned char* __restrict__ ws) {
    __shared__ __attribute__((aligned(16))) v4u Bl[56 * 64];
    const int b = blockIdx.x, tid = threadIdx.x, lane = tid & 63, w = __builtin_amdgcn_readfirstlane(tid >> 6);
    const int g = lane >> 4, r = lane & 15;
    const rsrc_t rs = __builtin_amdgcn_make_buffer_rsrc(ws, 0, WS_SPAN, 0x00020000);
    const int type = b >> 7, xc = b & 7, rem = (b >> 3) & 15;
    const int h = 8 * (xc >> 2) + (rem >> 1), j = 2 * ((xc >> 1) & 1) + (rem & 1), kh = xc & 1;
    v4u bq[8];
    v4u xv[7];
    if (w == 7) {
        const int row = 8 * b + (lane & 7);
#pragma unroll
        for (int it = 0; it < 7; ++it) xv[it] = *(const v4u*)(x + (size_t)row * 448 + ((lane >> 3) + 8 * it) * 8);
    } else if (type == 0) {
        const int tl = 7 * j + w, cc = 32 * (tl >> 1) + 8 * ((r >> 2) & 1) + 4 * (tl & 1) + (r & 3);
#pragma unroll
        for (int ke = 0; ke < 8; ++ke) bq[ke] = *(const v4u*)(Wq + (size_t)(cc + 16 * (ke & 1)) * 4096 + h * 256 + 64 * (ke >> 1) + 8 * (lane >> 3));
    } else {
        const int o = tid / 14, co = tid - 14 * o;
#pragma unroll
        for (int i = 0; i < 8; ++i) bq[i] = *(const v4u*)(Wo + (size_t)(h * 256 + 8 * o + i) * 448 + 112 * j + 8 * co);
    }
    v4u A[2][8];
    if (w < 7) {
        const u16* src = type ? Wv : Wk;
        const int ks = 7 * kh + w;
#pragma unroll
        for (int u = 0; u < 2; ++u) {
            const int d = 32 * ks + 8 * ((r >> 2) & 1) + 4 * u + (r & 3);
#pragma unroll
            for (int ke = 0; ke < 8; ++ke) A[u][ke] = *(const v4u*)(src + (size_t)(d + 16 * (ke & 1)) * 4096 + h * 256 + 64 * (ke >> 1) + 8 * (lane >> 3));
        }
    }
    {
        const v4u sv = {SENT, SENT, SENT, SENT};
        if (tid < 48) st4c(sv, rs, O_CH + (unsigned)(b * 48 + tid) * 16u, 0);
        {
            constexpr unsigned NF = 64 - FAST0, n0 = NF * 7168, n1 = 2 * n0, n2 = n1 + (64 - FASTA) * 7168, n3 = n2 + NF * 2048, n4 = n3 + NF * 512, n5 = n4 + (64 - FASTH) * 1792;
            for (unsigned u = (unsigned)b * 512u + (unsigned)tid; u < n5; u += 131072u) {
                const unsigned ad = u < n0 ? O_KMF + FAST0 * 114688u + u * 16u : u < n1 ? O_VMF + FAST0 * 114688u + (u - n0) * 16u
                                  : u < n2 ? O_ACT + FASTA * 114688u + (u - n1) * 16u : u < n3 ? O_PF + FAST0 * 32768u + (u - n2) * 16u
                                  : u < n4 ? O_PM + FAST0 * 8192u + (u - n3) * 16u : O_H1F + FASTH * 28672u + (u - n4) * 16u;
                st4c(sv, rs, ad, 0);
            }
        }
        if (w == 1 && lane < 2) *(unsigned*)(ws + O_FKE + (b * 2 + lane) * 128) = 0u;
        if (w == 0) { const v4u z = {0, 0, 0, 0}; *(v4u*)(ws + O_FLG + b * 1024 + lane * 16) = z; }
        if (b == 255 && tid < 448) {
            const int i = tid & 7, u = tid >> 3;
            *(v4u*)(ws + O_MEM0 + tid * 16) = *(const v4u*)(mem0 + i * 448 + u * 8);
        }
    }
    const int k = b >> 3;
    const unsigned pbase = O_CH + (unsigned)((k < 24 ? k : 0) >> 3) * (2 * MB) + (unsigned)((b & 7) * 8) * 32768u + (unsigned)(k & 7) * 4096u;
    if (w == 7) {
        if (k < 24 && lane < 4) st1c(SENT, rs, pbase + lane * 32768u, 0);
    } else if (type == 0) {
#pragma unroll
        for (int ke = 0; ke < 8; ke += 2) fix_pair(bq[ke], bq[ke + 1]);
#pragma unroll
        for (int ke = 0; ke < 8; ++ke) Bl[(w * 8 + ke) * 64 + g * 16 + ((r & 8) | ((r & 7) ^ ((2 * w + (r >> 3) + 4 * g + 6 * (ke & 1)) & 7)))] = bq[ke];
    } else {
        const int o = tid / 14, co = tid - 14 * o;
        v4u* dst = Bl + ((co >> 1) * 8 + 2 * (o >> 3) + (o & 1)) * 64 + ((o >> 1) & 3) * 16 + (co & 1) * 8;
#pragma unroll
        for (int cI = 0; cI < 8; ++cI) {
            v4u t;
#pragma unroll
            for (int q = 0; q < 4; ++q) t[q] = __builtin_amdgcn_perm(bq[2 * q + 1][cI >> 1], bq[2 * q][cI >> 1], (cI & 1) ? 0x07060302u : 0x05040100u);
            dst[cI ^ (tid & 7)] = t;
        }
    }
    __syncthreads();
    if (w < 7) {
        const float sc = type ? 1.0f : 0.0625f * 1.4426950408889634f;
        const int ks = 7 * kh + w;
#pragma unroll
        for (int ke = 0; ke < 8; ke += 2) { fix_pair(A[0][ke], A[0][ke + 1]); fix_pair(A[1][ke], A[1][ke + 1]); }
#pragma unroll
        for (int n = 0; n < 7; ++n) {
            f4 d0 = {0, 0, 0, 0}, d1 = {0, 0, 0, 0};
#pragma unroll
            for (int ke = 0; ke < 8; ++ke) {
                const v4u bb = Bl[(n * 8 + ke) * 64 + g * 16 + ((r & 8) | ((r & 7) ^ ((2 * n + (r >> 3) + 4 * g + 6 * (ke & 1)) & 7)))];
                d0 = mm32(A[0][ke], bb, d0);
                d1 = mm32(A[1][ke], bb, d1);
            }
            v4u o = {pk2(d0[0] * sc, d0[1] * sc), pk2(d0[2] * sc, d0[3] * sc), pk2(d1[0] * sc, d1[1] * sc), pk2(d1[2] * sc, d1[3] * sc)};
            __builtin_amdgcn_raw_buffer_store_b128(o, rs, O_WF + ((type * 448 + 28 * h + 7 * j + n) * 14 + ks) * 1024 + lane * 16, 0, SC1);
        }
    } else {
        float s = 0;
#pragma unroll
        for (int it = 0; it < 7; ++it)
#pragma unroll
            for (int q = 0; q < 4; ++q) s += lo16(xv[it][q]) + hi16(xv[it][q]);
        s += ror8(s); s = sw16add(s); s = sw32add(s);
        const float mean = s * (1.0f / 448.0f), mb = rbf(mean);
        float v = 0;
#pragma unroll
        for (int it = 0; it < 7; ++it)
#pragma unroll
            for (int q = 0; q < 4; ++q) { const float a = lo16(xv[it][q]) - mean, c = hi16(xv[it][q]) - mean; v += a * a + c * c; }
        v += ror8(v); v = sw16add(v); v = sw32add(v);
        const float den = rbf(sqrtf(rbf(rbf(v * (1.0f / 448.0f)) + 1e-5f)));
#pragma unroll
        for (int it = 0; it < 7; ++it)
#pragma unroll
            for (int q = 0; q < 4; ++q) xv[it][q] = pk2(rbf(lo16(xv[it][q]) - mb) / den, rbf(hi16(xv[it][q]) - mb) / den);
        unsigned xcc;
        asm volatile("s_getreg_b32 %0, hwreg(HW_REG_XCC_ID)" : "=s"(xcc));
        xcc &= 15u;
        unsigned best = 0xFFFFu;
        if (k < 24 && xcc < 4) {
            for (int i = 0; i < NPROBE; ++i) {
                const unsigned char* ad = ws + pbase + (i & 3) * 32768u;
                unsigned long long c0, c1; unsigned vv;
                asm volatile("s_waitcnt vmcnt(0)\n s_memtime %0\n s_waitcnt lgkmcnt(0)\n global_load_dword %2, %3, off sc1\n s_waitcnt vmcnt(0)\n s_memtime %1\n s_waitcnt lgkmcnt(0)"
                             : "=&s"(c0), "=&s"(c1), "=&v"(vv) : "v"(ad) : "memory");
                best = min(best, (unsigned)(c1 - c0));
            }
        }
#pragma unroll
        for (int it = 0; it < 7; ++it) {
            const int uu = (lane >> 3) + 8 * it;
            *(v4u*)(ws + O_XNF + (size_t)(b >> 1) * 14336 + (uu * 16 + (b & 1) * 8 + (lane & 7)) * 16) = xv[it];
        }
        if (lane == 0) *(unsigned*)(ws + O_CAL + b * 4) = (xcc << 16) | best;
    }
}

struct Ctx {
    rsrc_t rs; unsigned char* lds; int tid, lane, w, g, r;
};

DEV void pick_pages(const Ctx& c, unsigned& p0, unsigned& p1, unsigned& p2) {
    unsigned* tab = (unsigned*)(c.lds + 65536);
    if (c.tid < 24) tab[c.tid] = 0u;
    __syncthreads();
    if (c.tid < 192) {
        const unsigned rec = __builtin_amdgcn_raw_buffer_load_b32(c.rs, O_CAL + c.tid * 4, 0, 0);
        const unsigned xcc = rec >> 16, lat = rec & 0xFFFFu;
        if (xcc < 4) atomicAdd(&tab[c.tid >> 3], lat | (1u << 24));
    }
    __syncthreads();
    if (c.tid < 3) {
        unsigned used = 0, res = 0;
        const unsigned* t = tab + c.tid * 8;
        for (int q = 0; q < 4; ++q) {
            float bl = 0; int bi = -1;
            for (int p = 0; p < 8; ++p) {
                const float av = (t[p] >> 24) ? (float)(t[p] & 0xFFFFFFu) / (float)(t[p] >> 24) : 1e9f;
                if (!((used >> p) & 1) && (bi < 0 || av < bl)) { bl = av; bi = p; }
            }
            used |= 1u << bi; res |= (unsigned)bi << (3 * q);
        }
        tab[48 + c.tid] = res;
    }
    __syncthreads();
    p0 = __builtin_amdgcn_readfirstlane(tab[48]); p1 = __builtin_amdgcn_readfirstlane(tab[49]); p2 = __builtin_amdgcn_readfirstlane(tab[50]);
}
DEV unsigned pgsel(int slot, unsigned p0, unsigned p1, unsigned p2) { return slot < 21 ? p0 : (slot < 42 ? p1 : p2); }
template <int LG, bool TWO>
DEV void mark_empty(const Ctx& c, unsigned p0, unsigned p1, unsigned p2, int q, unsigned off, int q2, unsigned off2) {
    const v4u sv = {SENT, SENT, SENT, SENT};
    for (int i = c.tid; i < (61 << LG); i += 512) {
        const int sl = 2 + (i >> LG);
        const unsigned pgw = pgsel(sl, p0, p1, p2), o = (unsigned)(i & ((1 << LG) - 1)) * 16u;
        st4c(sv, c.rs, page_addr(pgw, sl, q) + off + o, 0);
        if (TWO) st4c(sv, c.rs, page_addr(pgw, sl, q2) + off2 + o, 0);
    }
    vm0();
    __syncthreads();
}

#define PIN_S(x) asm volatile("" : "+s"(x))
#define PIN_V(x) asm volatile("" : "+v"(x))
DEV void role_score(const Ctx& c, int id) {
    const int h = id >> 2, j = id & 3, w = c.w, lane = c.lane, g = c.g, n8 = lane & 7;
    unsigned char* red = c.lds;
    unsigned char* red2 = c.lds + 16384;
    int budget = 1 << 17;
    if (c.tid < 64) *(unsigned*)(red + 1024 + c.tid * 16 + 12) = 0u;
    const int odd = j & 1, cf = (7 * j + 1) >> 1, tx = odd ? 7 * j : 7 * j + 6;
    v4u W[14];
    if (w < 7) {
#pragma unroll
        for (int n = 0; n < 7; ++n) {
            const int tg = n < 6 ? 2 * cf + n : tx;
#pragma unroll
            for (int kk = 0; kk < 2; ++kk) W[n * 2 + kk] = ld4(c.rs, O_WF + ((28 * h + tg) * 14 + 2 * w + kk) * 1024 + lane * 16, 0);
        }
    }
    unsigned p0, p1, p2;
    pick_pages(c, p0, p1, p2);
    mark_empty<4, false>(c, p0, p1, p2, id >> 4, (unsigned)(id & 15) * 256u, 0, 0u);
    if (w == 7) __builtin_amdgcn_endpgm();
    const int b3 = (lane >> 3) & 1;
    const unsigned fo = (unsigned)(g * 128 + n8 * 16), fo2 = fo + (unsigned)b3 * 512u;
    unsigned* myred = (unsigned*)(g < 2 ? red + (w >> 2) * 1024 + ((g * 8 + n8) * 4 + 2 * b3) * 16 + (w & 3) * 4 : red + 2048 + lane * 32);
    unsigned mbase = O_MEM0 + (unsigned)w * 1024u;
    unsigned qa0 = O_MEM0 + (unsigned)(cf + b3) * 512u + fo, qa1 = O_MEM0 + (unsigned)(b3 ? (tx >> 1) * 512 + odd * 8 : (cf + 2) * 512) + fo;
    const int rep = 7 * (id >> 5);
    vm0();
    for (int s = 0; s < 64; ++s) {
        const int sl = s < 63 ? s : 62;
        const unsigned pgw = pgsel(sl, p0, p1, p2);
        unsigned sad = page_addr(pgw, sl, id >> 4) + (unsigned)(id & 15) * 256u;
        unsigned mnext = mem_addr(pgw, sl, rep + w);
        unsigned qn[2];
#pragma unroll
        for (int t = 0; t < 2; ++t) {
            const int ca = cf + 2 * t, cb = t ? tx >> 1 : cf + 1;
            const unsigned aa = mem_addr(pgw, sl, rep + (ca >> 1)) + (unsigned)(ca & 1) * 512u;
            const unsigned ab = mem_addr(pgw, sl, rep + (cb >> 1)) + (unsigned)((cb & 1) * 512 + (t ? odd * 8 : 0));
            qn[t] = (b3 ? ab : aa) + fo;
        }
        PIN_S(sad); PIN_S(mnext); PIN_V(qn[0]); PIN_V(qn[1]);
        TMD;
        v4u b0, q0, q1;
        for (;;) {
            MEMBAR();
            b0 = ld4c(c.rs, fo2, mbase);
            if (__all(umx(b0) != SENT)) break;
            if (--budget < 0) break;
        }
        TS(s, 0); TM(0);
        MEMBAR();
        q0 = ld4c(c.rs, qa0, 0); q1 = ld4c(c.rs, qa1, 0);
        __builtin_amdgcn_sched_barrier(0);
        f4 T[7];
#pragma unroll
        for (int n = 0; n < 7; ++n) T[n] = mm32(W[2 * n], b0, (f4){0, 0, 0, 0});
        const v4u b1 = {ror8u(b0.x), ror8u(b0.y), ror8u(b0.z), ror8u(b0.w)};
#pragma unroll
        for (int n = 0; n < 7; ++n) T[n] = mm32(W[2 * n + 1], b1, T[n]);
        __builtin_amdgcn_sched_barrier(0);
        if (s < 63) {
            for (;;) {
                if (__all(max(umx(q0), umx(q1)) != SENT)) break;
                if (--budget < 0) break;
                MEMBAR();
                q0 = ld4c(c.rs, qa0, 0); q1 = ld4c(c.rs, qa1, 0);
            }
            TS(s, 1); TM(2);
            const v2u mq[7] = {{q0.x, q0.y}, {q0.z, q0.w}, {q0.x, q0.y}, {q0.z, q0.w}, {q1.x, q1.y}, {q1.z, q1.w}, {q1.x, q1.y}};
            f4 Sa = {0, 0, 0, 0}, Sb = {0, 0, 0, 0};
#pragma unroll
            for (int i = 0; i < 7; ++i) {
                const int n = (i >> 2) * 4 + (i & 1) * 2 + ((i >> 1) & 1);
                const v2u a = {pk2(T[n][0], T[n][1]), pk2(T[n][2], T[n][3])};
                if (n & 2) Sb = mm16(a, mq[n], Sb); else Sa = mm16(a, mq[n], Sa);
            }
            myred[0] = __float_as_uint((b3 ? Sb[2] : Sa[0]) + ror8(b3 ? Sb[0] : Sa[2]));
            myred[4] = __float_as_uint((b3 ? Sb[3] : Sa[1]) + ror8(b3 ? Sb[1] : Sa[3]));
            bar_lds();
            if (w == 0) {
                const f4 x0 = *(const f4*)(red + lane * 16), x1 = *(const f4*)(red + 1024 + lane * 16);
                const f4 y = x0 + x1;
                st1c(clampS(__float_as_uint((y[0] + y[1]) + (y[2] + y[3]))), c.rs, lane * 4, sad);
                TS(s, 2); TM(5);
                MEMBAR();
            }
        } else bar_lds();
        if ((lane & 8) == 0) {
#pragma unroll
            for (int n = 0; n < 7; ++n) *(f4*)(red2 + (w * 7 + n) * 512 + (g * 8 + n8) * 16) = T[n];
        }
        if (s > 0 && w >= 1 && w <= 4) {
            vm0();
            if (lane == 0) st1c(1u, c.rs, O_FKM + (((s - 1) * 16 + h) * 16 + j * 4 + (w - 1)) * 4, 0);
        }
        bar_lds();
        TS(s, 3);
        if (w >= 1 && w <= 4) {
            const int t = c.tid - 64;
            if (t < 224) {
                const int n = t >> 5, q = t & 31;
                f4 acc = *(const f4*)(red2 + n * 512 + q * 16);
#pragma unroll
                for (int ww = 1; ww < 7; ++ww) acc += *(const f4*)(red2 + (ww * 7 + n) * 512 + q * 16);
                const int tg = n < 6 ? 2 * cf + n : tx;
                const v2u o = {pk2c(acc[0], acc[1]), pk2c(acc[2], acc[3])};
                st2c(o, c.rs, O_KMF + ((s * 16 + h) * 14 + (tg >> 1)) * 512 + q * 16 + (tg & 1) * 8, 0);
            }
        }
        mbase = mnext; qa0 = qn[0]; qa1 = qn[1];
        TMW(s);
        DELAY(0, PRE_S);
    }
    vm0();
    if (w >= 1 && w <= 4 && lane == 0) st1c(1u, c.rs, O_FKM + ((63 * 16 + h) * 16 + j * 4 + (w - 1)) * 4, 0);
}

DEV void role_tok(const Ctx& c, int e) {
    const int w = c.w, lane = c.lane, g = c.g, n8 = lane & 7, b3 = (lane >> 3) & 1;
    const int h = 2 * e + ((w >> 1) & 1), tile = w & 1, rep = 7 * (e >> 2);
    const int grp = w >> 2, ka = 2 * (w & 3), kb = ka < 6 ? ka + 1 : 6;
    unsigned char* memL = c.lds + grp * 8192;
    int budget = 1 << 17;
    unsigned p0, p1, p2;
    pick_pages(c, p0, p1, p2);
    const unsigned fo = (unsigned)(g * 128 + n8 * 16), fo2 = fo + (unsigned)b3 * 512u;
    const v4u sv = {SENT, SENT, SENT, SENT};
    const unsigned eoff = (unsigned)((h & 3) * 1024 + tile * 512);
    if (!b3) st4c(sv, c.rs, fo, page_addr(p0, grp + 2, 4 + (h >> 2)) + eoff);
    v4u K[14];
    if (grp) bar_lds();
    for (int s = grp; s < 63; s += 2) {
        wait_flags(c.rs, fkv(s, lane), lane < 28, budget);
#pragma unroll
        for (int i = 0; i < 14; ++i) K[i] = ld4c(c.rs, lane * 16, O_KF + ((s * 16 + h) * 28 + tile * 14 + i) * 1024);
        const unsigned pgm = pgsel(s - 1, p0, p1, p2);
        unsigned ma = s > 0 ? mem_addr(pgm, s - 1, rep + ka) : O_MEM0 + (unsigned)ka * 1024u;
        unsigned mb = s > 0 ? mem_addr(pgm, s - 1, rep + kb) : O_MEM0 + (unsigned)kb * 1024u;
        unsigned ead = page_addr(pgsel(s, p0, p1, p2), s, 4 + (h >> 2)) + eoff;
        PIN_S(ma); PIN_S(mb); PIN_S(ead);
        TMD;
        if (s > 1) bar_lds();
        if (s > 3) DELAY(3, PRE_E);
        vm0();
        v4u x0 = ld4c(c.rs, lane * 16, ma), x1 = ld4c(c.rs, lane * 16, mb);
        for (;;) {
            const unsigned u0 = umx(x0), u1 = umx(x1);
            if (__all(max(u0, u1) != SENT)) break;
            if (--budget < 0) break;
            MEMBAR();
            if (__any(u0 == SENT)) x0 = ld4c(c.rs, lane * 16, ma);
            if (__any(u1 == SENT)) x1 = ld4c(c.rs, lane * 16, mb);
        }
        *(v4u*)(memL + ka * 1024 + lane * 16) = x0;
        *(v4u*)(memL + (ka + 1) * 1024 + lane * 16) = x1;
        bar_lds();
        TS(s, 0); TM(0);
        v4u b[7];
#pragma unroll
        for (int k = 0; k < 7; ++k) b[k] = *(const v4u*)(memL + k * 1024 + fo2);
        __builtin_amdgcn_sched_barrier(0);
        f4 A0 = {0, 0, 0, 0}, A1 = {0, 0, 0, 0};
#pragma unroll
        for (int k = 0; k < 7; ++k) { A0 = mm32(K[2 * k], b[k], A0); A1 = mm32(K[2 * k + 1], b[k], A1); }
        float v[4];
#pragma unroll
        for (int q = 0; q < 4; ++q) { v[q] = b3 ? A1[q] : A0[q]; v[q] += ror8(v[q]); }
        const float mx = sw32max(sw16max(fmaxf(fmaxf(v[0], v[1]), fmaxf(v[2], v[3]))));
        const float e0 = ex2(v[0] - mx), e1 = ex2(v[1] - mx), e2 = ex2(v[2] - mx), e3 = ex2(v[3] - mx);
        const float l = sw32add(sw16add((e0 + e1) + (e2 + e3)));
        const v4u o = {pk2(e0, e1), pk2(e2, e3), clampS(__float_as_uint(mx)), clampS(__float_as_uint(l))};
        if (!b3) st4c(o, c.rs, fo, ead);
        __builtin_amdgcn_sched_barrier(0);
        if (!b3 && s < 59) st4c(sv, c.rs, fo, page_addr(pgsel(s + 4, p0, p1, p2), s + 4, 4 + (h >> 2)) + eoff);
        TS(s, 2); TM(3);
        TMW(s);
    }
    if (grp) bar_lds();
}

typedef unsigned v8u __attribute__((ext_vector_type(8)));
typedef __bf16 bf16v __attribute__((ext_vector_type(16)));
DEV f4 sm64(v4u a, v8u b, f4 c, int idx) { return __builtin_amdgcn_smfmac_f32_16x16x64_bf16(__builtin_bit_cast(bf8, a), __builtin_bit_cast(bf16v, b), c, idx, 0, 0); }
DEV unsigned to_half(unsigned x) { x = __builtin_amdgcn_update_dpp(x, x, 0x128, 0x3, 0xc, false); return __builtin_amdgcn_update_dpp(x, x, 0x128, 0xc, 0x3, false); }
DEV void role_out(const Ctx& c, int ct) {
    const int w = c.w, lane = c.lane, g = c.g, n8 = lane & 7, hh = lane >> 5;
    unsigned char* memL = c.lds;
    unsigned char* red = c.lds + 8192;
    unsigned char* ybuf = c.lds + 12288;
    int budget = 1 << 17;
    v8u W[14];
#pragma unroll
    for (int a = 0; a < 2; ++a)
#pragma unroll
        for (int i = 0; i < 7; ++i)
#pragma unroll
            for (int z = 0; z < 2; ++z) {
                const v4u t = ld4(c.rs, O_WF + ((448 + 28 * (2 * w + a) + ct) * 14 + 2 * i + z) * 1024 + (c.r + 16 * (2 * (g & 1) + (g >> 1))) * 16, 0);
                const auto e0 = __builtin_amdgcn_permlane16_swap(t.x, t.z, false, false), e1 = __builtin_amdgcn_permlane16_swap(t.y, t.w, false, false);
                W[a * 7 + i][z] = e0[0]; W[a * 7 + i][2 + z] = e1[0];
                W[a * 7 + i][4 + z] = e0[1]; W[a * 7 + i][6 + z] = e1[1];
            }
    unsigned p0, p1, p2;
    pick_pages(c, p0, p1, p2);
    mark_empty<4, true>(c, p0, p1, p2, 8 + (ct >> 4), (unsigned)(((ct >> 2) & 3) * 1024 + (ct & 3) * 256), 8 + ((7 + (ct >> 2)) >> 2),
                        (unsigned)(((7 + (ct >> 2)) & 3) * 1024 + (ct & 3) * 256));
    const int b3 = (lane >> 3) & 1;
    const unsigned fo = (unsigned)(g * 128 + n8 * 16);
    const unsigned so = (unsigned)((hh * 4 + b3) * 256 + ((g & 1) * 8 + n8) * 16);
    const unsigned oo = (unsigned)((ct & 3) >> 1) * 512u + (unsigned)(ct & 1) * 256u + (unsigned)((g & 1) * 128 + n8 * 16 + b3 * 8);
    const unsigned eo = fo + (unsigned)b3 * 512u, to = fo + (unsigned)b3 * 1024u;
    const int rep = ct < 14 ? 0 : 7;
    const int idx = b3 ? 0xEEEE : 0x4444;
    v4u V0, V1;
    unsigned kvf = 0;
    const int ws1 = w > 0 ? w - 1 : 0;
    unsigned mbase = O_MEM0 + (unsigned)ws1 * 1024u;
    vm0();
    for (int s = 0; s < 64; ++s) {
        const int sl = s < 63 ? s : 62;
        const unsigned pgw = pgsel(sl, p0, p1, p2);
        unsigned sad = page_addr(pgw, sl, w >> 1) + (unsigned)(w & 1) * 2048u;
        unsigned ead = page_addr(pgw, sl, 4 + (w >> 1)) + (unsigned)(w & 1) * 2048u;
        unsigned oav = (hh ? mem_addr(pgw, sl, 7 + (ct >> 2)) : mem_addr(pgw, sl, ct >> 2)) + oo;
        unsigned mnext = mem_addr(pgw, sl, rep + ws1);
        PIN_S(sad); PIN_S(ead); PIN_S(mnext); PIN_V(oav);
        TMD;
        if (w > 0) {
            v4u b0;
            for (;;) {
                MEMBAR();
                b0 = ld4c(c.rs, lane * 16, mbase);
                if (__all(umx(b0) != SENT)) break;
                if (--budget < 0) break;
            }
            *(v4u*)(memL + ws1 * 1024 + lane * 16) = b0;
            TS(s, 0); TM(0);
        }
        bar_lds();
        TM(1);
        v4u m[7];
#pragma unroll
        for (int i = 0; i < 7; ++i) m[i] = *(const v4u*)(memL + i * 1024 + eo);
        __builtin_amdgcn_sched_barrier(0);
        if (s < 63) {
            if (!__all(kvf != 0)) wait_flags(c.rs, fkv(s, 28 + lane), lane < 28, budget);
            MEMBAR();
            V0 = ld4c(c.rs, O_VF + ((s * 16 + 2 * w) * 28 + ct) * 1024 + lane * 16, 0);
            V1 = ld4c(c.rs, O_VF + ((s * 16 + 2 * w + 1) * 28 + ct) * 1024 + lane * 16, 0);
            kvf = ld1c(c.rs, fkv(s + 1, 28 + (lane < 28 ? lane : 0)), 0);
        }
        __builtin_amdgcn_sched_barrier(0);
        f4 Y0 = {0, 0, 0, 0}, Y1 = {0, 0, 0, 0};
#pragma unroll
        for (int i = 0; i < 7; ++i) {
            Y0 = sm64(m[i], W[i], Y0, idx);
            Y1 = sm64(m[i], W[7 + i], Y1, idx);
        }
        float yv[4];
#pragma unroll
        for (int q = 0; q < 4; ++q) {
            const auto r = __builtin_amdgcn_permlane32_swap(__float_as_uint(Y0[q]), __float_as_uint(Y1[q]), false, false);
            yv[q] = __uint_as_float(r[0]) + __uint_as_float(r[1]);
        }
        v2u Yp = {pk2c(yv[0], yv[1]), pk2c(yv[2], yv[3])};
        *(v2u*)(ybuf + w * 512 + lane * 8) = Yp;
        PIN_V(Yp);
        if (s < 63) {
            TS(s, 1); TM(2);
            DELAY(2, PRE_O);
            MEMBAR();
            v4u Ra = ld4c(c.rs, to, ead);
            v4u Rb = ld4c(c.rs, to + 512, ead);
            for (;;) {
                const unsigned ra = umx(Ra), rb = umx(Rb);
                if (__all(max(ra, rb) != SENT)) break;
                if (--budget < 0) break;
                MEMBAR();
                if (__any(ra == SENT)) Ra = ld4c(c.rs, to, ead);
                if (__any(rb == SENT)) Rb = ld4c(c.rs, to + 512, ead);
            }
            vm0();
            DELAY(4, PRE_P);
            v4u Sa = ld4c(c.rs, so, sad);
            v4u Sb = ld4c(c.rs, so + 512, sad);
            __builtin_amdgcn_sched_barrier(0);
            const f4 z = {0, 0, 0, 0};
            const v2u Pa = {Ra.x, Ra.y}, Pb = {Rb.x, Rb.y};
            const f4 U0a = mm16((v2u){V0.x, V0.y}, Pa, z), U1a = mm16((v2u){V1.x, V1.y}, Pa, z);
            const f4 U0b = mm16((v2u){V0.z, V0.w}, Pb, z), U1b = mm16((v2u){V1.z, V1.w}, Pb, z);
            const float mb = fmaxf(__uint_as_float(Ra.z), __uint_as_float(Rb.z));
            const float w0 = ex2(__uint_as_float(Ra.z) - mb), w1 = ex2(__uint_as_float(Rb.z) - mb);
            float mt = __uint_as_float(to_half(__float_as_uint(mb)));
            float lt = __uint_as_float(to_half(__float_as_uint(w0 * __uint_as_float(Ra.w) + w1 * __uint_as_float(Rb.w))));
            f4 Ut;
#pragma unroll
            for (int q = 0; q < 4; ++q) Ut[q] = (b3 ? U1a[q] : U0a[q]) * w0 + (b3 ? U1b[q] : U0b[q]) * w1;
            PIN_V(Ut); PIN_V(mt); PIN_V(lt);
            __builtin_amdgcn_sched_barrier(0);
            for (;;) {
                const unsigned ma = umx(Sa), mb2 = umx(Sb);
                if (__all(max(ma, mb2) != SENT)) break;
                if (--budget < 0) break;
                MEMBAR();
                if (__any(ma == SENT)) Sa = ld4c(c.rs, so, sad);
                if (__any(mb2 == SENT)) Sb = ld4c(c.rs, so + 512, sad);
            }
            TS(s, 2); TM(3);
            float sv[4];
#pragma unroll
            for (int q = 0; q < 4; ++q) { sv[q] = __uint_as_float(Sa[q]) + __uint_as_float(Sb[q]); sv[q] += ror8(sv[q]); }
            const float M = sw16max(fmaxf(fmaxf(fmaxf(sv[0], sv[1]), mt), fmaxf(sv[2], sv[3])));
            const float e0 = ex2(sv[0] - M), e1 = ex2(sv[1] - M), e2 = ex2(sv[2] - M), e3 = ex2(sv[3] - M);
            const float wt = ex2(mt - M);
            const float L = sw16add((e0 + e1) + (e2 + e3)) + wt * lt;
            const float rl = __builtin_amdgcn_rcpf(L);
            const v2u P = {pk2(e0 * rl, e1 * rl), pk2(e2 * rl, e3 * rl)};
            const f4 D = mm16(Yp, P, z);
            const auto fs = __builtin_amdgcn_permlane32_swap(__float_as_uint(wt * rl), __float_as_uint(wt * rl), false, false);
            const float ft = __uint_as_float(b3 ? fs[1] : fs[0]);
            f4 acc;
#pragma unroll
            for (int q = 0; q < 4; ++q) { acc[q] = Ut[q] * ft; acc[q] += ror8(acc[q]); }
            acc += D;
            if ((lane & 8) == 0) *(f4*)(red + w * 512 + (g * 8 + n8) * 16) = acc;
            TM(4);
            bar_lds();
            TM(5);
            if (w == 0) {
                const unsigned char* rp = red + hh * 2048 + (lane & 31) * 16;
                f4 t = (*(const f4*)(rp) + *(const f4*)(rp + 512)) + (*(const f4*)(rp + 1024) + *(const f4*)(rp + 1536));
                const float t0 = sw32add(t[0]), t1 = sw32add(t[1]), t2 = sw32add(t[2]), t3 = sw32add(t[3]);
                const v2u o = {clampS(pk2(t0, t1)), clampS(pk2(t2, t3))};
                st2c(o, c.rs, oav, 0);
                TS(s, 3); TM(6);
            }
        } else { vm0(); bar_lds(); }
        if (w == 0) {
            MEMBAR();
            if (s > 0 && lane == 0) st1c(1u, c.rs, O_FVM + ((s - 1) * 32 + ct) * 32, 0);
#pragma unroll
            for (int i = 0; i < 4; ++i)
                st4c(*(const v4u*)(ybuf + i * 1024 + lane * 16), c.rs, O_VMF + ((s * 16 + 4 * i + g) * 28 + ct) * 256 + c.r * 16, 0);
        }
        mbase = mnext;
        TMW(s);
        DELAY(1, PRE_M);
    }
    vm0();
    if (c.tid == 0) st1c(1u, c.rs, O_FVM + (63 * 32 + ct) * 32, 0);
}

template <bool ISK>
DEV void role_kv(const Ctx& c, int k) {
    const int w = c.w, lane = c.lane, tid = c.tid;
    const int p = 8 * k + w;
    v4u W[28];
#pragma unroll
    for (int i = 0; i < 28; ++i) W[i] = ld4(c.rs, O_WF + (2 * p * 14 + i) * 1024 + lane * 16, 0);
    v4u* xl = (v4u*)c.lds;
#pragma unroll
    for (int i = 0; i < 4; ++i) if (tid + 512 * i < 1792) xl[tid + 512 * i] = ld4(c.rs, O_XNF + (tid + 512 * i) * 16, 0);
    __syncthreads();
    const int pp = ISK ? p : p - 224, h = pp / 14, t = pp - 14 * h;
    for (int s = 0; s < 64; ++s) {
        v4u pre[4];
        if (s < 63) {
#pragma unroll
            for (int i = 0; i < 4; ++i) if (tid + 512 * i < 1792) pre[i] = ld4(c.rs, O_XNF + (s + 1) * 28672 + (tid + 512 * i) * 16, 0);
        }
        const v4u* xb = xl + (s & 1) * 1792 + lane;
        f4 a00 = {0, 0, 0, 0}, a01 = a00, a10 = a00, a11 = a00;
#pragma unroll
        for (int ks = 0; ks < 14; ++ks) {
            const v4u x0 = xb[ks * 64], x1 = xb[896 + ks * 64];
            if (ISK) {
                a00 = mm32(W[ks], x0, a00); a10 = mm32(W[14 + ks], x0, a10);
                a01 = mm32(W[ks], x1, a01); a11 = mm32(W[14 + ks], x1, a11);
            } else {
                a00 = mm32(x0, W[ks], a00); a10 = mm32(x0, W[14 + ks], a10);
                a01 = mm32(x1, W[ks], a01); a11 = mm32(x1, W[14 + ks], a11);
            }
        }
        TS(s, 0);
        vm0();
        TS(s, 1);
        if (s < 63) {
            v4u* xn = xl + ((s + 1) & 1) * 1792;
#pragma unroll
            for (int i = 0; i < 4; ++i) if (tid + 512 * i < 1792) xn[tid + 512 * i] = pre[i];
        }
        if (ISK) {
            const v4u o0 = {pk2(a00[0], a00[1]), pk2(a00[2], a00[3]), pk2(a10[0], a10[1]), pk2(a10[2], a10[3])};
            const v4u o1 = {pk2(a01[0], a01[1]), pk2(a01[2], a01[3]), pk2(a11[0], a11[1]), pk2(a11[2], a11[3])};
            const unsigned ob = O_KF + (((s * 16 + h) * 2) * 14 + t) * 1024 + lane * 16;
            st4c(o0, c.rs, ob, 0);
            st4c(o1, c.rs, ob + 14336, 0);
        } else {
            const v4u o0 = {pk2(a00[0], a00[1]), pk2(a00[2], a00[3]), pk2(a01[0], a01[1]), pk2(a01[2], a01[3])};
            const v4u o1 = {pk2(a10[0], a10[1]), pk2(a10[2], a10[3]), pk2(a11[0], a11[1]), pk2(a11[2], a11[3])};
            const unsigned ob = O_VF + ((s * 16 + h) * 28 + 2 * t) * 1024 + lane * 16;
            st4c(o0, c.rs, ob, 0);
            st4c(o1, c.rs, ob + 1024, 0);
        }
        if (s < 3 || s == 63) vm0();
        __syncthreads();
        TS(s, 2);
        if (tid == 0) {
            if (s > 0) st1c(1u, c.rs, fkv(s - 1, k), 0);
            if (s < 3 || s == 63) st1c(1u, c.rs, fkv(s, k), 0);
        }
    }
}

DEV void role_atts(const Ctx& c, int a) {
    const int lane = c.lane, g = c.g, q = c.r;
    const int gw = 8 * a + c.w, h = gw & 15;
    int budget = 1 << 16;
    const unsigned fo = (unsigned)(g * 128 + (lane & 7) * 16);
    for (int seg = gw >> 4; seg < 64; seg += 8) {
        v4u X[28];
#pragma unroll
        for (int i = 0; i < 28; ++i) X[i] = ld4(c.rs, lane * 16, O_XNF + (seg * 28 + i) * 1024);
        wait_flags(c.rs, fkv(seg, lane), lane < 56, budget);
        f4 s00 = {0, 0, 0, 0}, s01 = s00, s11 = s00, m0 = s00, m1 = s00;
        const unsigned kb = O_KF + ((seg * 16 + h) * 28) * 1024;
#pragma unroll
        for (int hf = 0; hf < 2; ++hf) {
            v4u K[14];
#pragma unroll
            for (int i = 0; i < 7; ++i) { K[i] = ld4c(c.rs, lane * 16, kb + (7 * hf + i) * 1024); K[7 + i] = ld4c(c.rs, lane * 16, kb + (14 + 7 * hf + i) * 1024); }
            __builtin_amdgcn_sched_barrier(0);
#pragma unroll
            for (int i = 0; i < 7; ++i) {
                s00 = mm32(K[i], X[7 * hf + i], s00);
                s01 = mm32(K[i], X[14 + 7 * hf + i], s01);
                s11 = mm32(K[7 + i], X[14 + 7 * hf + i], s11);
            }
        }
        TS(seg, 0);
        wait_flags(c.rs, O_FKM + (((seg < FAST0 ? seg : seg - 2) * 16 + h) * 16 + lane) * 4, lane < 16, budget);
        if (seg >= FAST0) __builtin_amdgcn_s_sleep(40);
        {
            v4u K[14];
            for (;;) {
                MEMBAR();
#pragma unroll
                for (int i = 0; i < 14; ++i) K[i] = ld4c(c.rs, fo, O_KMF + ((seg * 16 + h) * 14 + i) * 512);
                if (seg < FAST0) break;
                unsigned mx = 0;
#pragma unroll
                for (int i = 0; i < 14; ++i) mx = max(mx, umx(K[i]));
                if (__all(mx != SENT)) break;
                if (--budget < 0) break;
                FSLEEP;
            }
            TS(seg, 1);
            __builtin_amdgcn_sched_barrier(0);
#pragma unroll
            for (int i = 0; i < 14; ++i) { m0 = mm32(K[i], X[i], m0); m1 = mm32(K[i], X[14 + i], m1); }
        }
        const float NEG = -3.0e38f;
#pragma unroll
        for (int i = 0; i < 4; ++i) {
            if (g >= 2) { m0[i] = NEG; m1[i] = NEG; }
            if (4 * g + i > q) { s00[i] = NEG; s11[i] = NEG; }
        }
        const unsigned pb = (unsigned)((seg * 16 + h) * 2);
        {
            float M = fmaxf(fmaxf(fmaxf(m0[0], m0[1]), fmaxf(m0[2], m0[3])), fmaxf(fmaxf(s00[0], s00[1]), fmaxf(s00[2], s00[3])));
            M = sw32max(sw16max(M));
            float L = 0;
#pragma unroll
            for (int i = 0; i < 4; ++i) { m0[i] = ex2(m0[i] - M); s00[i] = ex2(s00[i] - M); L += m0[i] + s00[i]; }
            L = sw32add(sw16add(L));
            const float rl = 1.0f / L;
            const v4u o = {pk2c(s00[0] * rl, s00[1] * rl), pk2c(s00[2] * rl, s00[3] * rl), 0u, 0u};
            st4c(o, c.rs, O_PF + pb * 1024 + lane * 16, 0);
            const v2u om = {pk2c(m0[0] * rl, m0[1] * rl), pk2c(m0[2] * rl, m0[3] * rl)};
            if (lane < 32) st2c(om, c.rs, O_PM + pb * 256 + lane * 8, 0);
        }
        {
            float M = fmaxf(fmaxf(fmaxf(m1[0], m1[1]), fmaxf(m1[2], m1[3])), fmaxf(fmaxf(s01[0], s01[1]), fmaxf(s01[2], s01[3])));
            M = fmaxf(M, fmaxf(fmaxf(s11[0], s11[1]), fmaxf(s11[2], s11[3])));
            M = sw32max(sw16max(M));
            float L = 0;
#pragma unroll
            for (int i = 0; i < 4; ++i) { m1[i] = ex2(m1[i] - M); s01[i] = ex2(s01[i] - M); s11[i] = ex2(s11[i] - M); L += m1[i] + s01[i] + s11[i]; }
            L = sw32add(sw16add(L));
            const float rl = 1.0f / L;
            const v4u o = {pk2c(s01[0] * rl, s01[1] * rl), pk2c(s01[2] * rl, s01[3] * rl), pk2c(s11[0] * rl, s11[1] * rl), pk2c(s11[2] * rl, s11[3] * rl)};
            st4c(o, c.rs, O_PF + (pb + 1) * 1024 + lane * 16, 0);
            const v2u om = {pk2c(m1[0] * rl, m1[1] * rl), pk2c(m1[2] * rl, m1[3] * rl)};
            if (lane < 32) st2c(om, c.rs, O_PM + (pb + 1) * 256 + lane * 8, 0);
        }
        vm0();
        if (lane == 0) st1c(1u, c.rs, O_FP + (seg * 16 + h) * 4, 0);
        TS(seg, 3);
    }
}

DEV f4 red8(const unsigned char* rp) {
    f4 t0 = *(const f4*)(rp) + *(const f4*)(rp + 2048), t1 = *(const f4*)(rp + 4096) + *(const f4*)(rp + 6144);
    f4 t2 = *(const f4*)(rp + 8192) + *(const f4*)(rp + 10240), t3 = *(const f4*)(rp + 12288) + *(const f4*)(rp + 14336);
    return (t0 + t1) + (t2 + t3);
}

DEV void role_atto(const Ctx& c, int o, const u16* __restrict__ x) {
    const int w = c.w, lane = c.lane, g = c.g, q = c.r, hh = lane >> 5;
    const int cp = o >> 1, qt = o & 1;
    int budget = 1 << 16;
    for (int seg = 0; seg < 64; ++seg) {
        const int tile = 2 * seg + qt;
        v2u xr = {0, 0};
        if (w < 2) xr = *(const v2u*)(x + (size_t)(tile * 16 + q) * 448 + 32 * cp + 16 * w + 4 * g);
        const unsigned fa = seg >= FAST0 ? O_FKV + (seg * 64 + 28 + (lane < 28 ? lane : 0)) * 4
                          : (lane & 2) == 0 ? O_FP + (seg * 16 + 2 * w + (lane & 1)) * 4 : O_FVM + (seg * 32 + 2 * cp + (lane & 1)) * 32;
        MEMBAR();
        const unsigned f0 = ld1c(c.rs, fa, 0);
        vm0();
        if (seg > 0 && w < 2 && lane == 0) st1c(1u, c.rs, O_FH1 + ((tile - 2) * 32 + 2 * cp + w) * 4, 0);
        if (!__all(f0 != 0)) wait_flags(c.rs, fa, true, budget);
        MEMBAR();
        const unsigned vb = O_VF + ((seg * 16 + 2 * w) * 28 + 2 * cp) * 1024 + lane * 16;
        const v4u V00 = ld4c(c.rs, vb, 0), V01 = ld4c(c.rs, vb + 1024, 0), V10 = ld4c(c.rs, vb + 28672, 0), V11 = ld4c(c.rs, vb + 28672 + 1024, 0);
        const unsigned pb = O_PF + ((seg * 16 + 2 * w) * 2 + qt) * 1024 + lane * 16;
        const unsigned vm = O_VMF + ((seg * 16 + 2 * w + hh) * 28 + 2 * cp) * 256 + (lane & 31) * 8;
        v4u P0, P1;
        v2u M0, M1, Pm;
        for (;;) {
            MEMBAR();
            P0 = ld4c(c.rs, pb, 0); P1 = ld4c(c.rs, pb + 2048, 0);
            M0 = ld2c(c.rs, vm, 0); M1 = ld2c(c.rs, vm + 256, 0);
            Pm = ld2c(c.rs, O_PM + ((seg * 16 + 2 * w + hh) * 2 + qt) * 256 + (lane & 31) * 8, 0);
            if (seg < FAST0) break;
            if (__all(max(max(umx(P0), umx(P1)), max(max(umx2(M0), umx2(M1)), umx2(Pm))) != SENT)) break;
            if (--budget < 0) break;
            FSLEEP;
        }
        TS(seg, 0);
        __builtin_amdgcn_sched_barrier(0);
        f4 a0 = {0, 0, 0, 0}, a1 = a0;
        a0 = mm32(V00, P0, a0); a1 = mm32(V01, P0, a1);
        a0 = mm32(V10, P1, a0); a1 = mm32(V11, P1, a1);
        a0 = mm16(M0, Pm, a0); a1 = mm16(M1, Pm, a1);
        unsigned char* red = c.lds + (seg & 1) * 16384;
        *(f4*)(red + w * 2048 + lane * 16) = a0;
        *(f4*)(red + w * 2048 + 1024 + lane * 16) = a1;
        __syncthreads();
        if (w < 2) {
            const f4 t = red8(red + w * 1024 + lane * 16);
            const v2u ov = {pk2c(lo16(xr.x) + rbf(t[0]), hi16(xr.x) + rbf(t[1])), pk2c(lo16(xr.y) + rbf(t[2]), hi16(xr.y) + rbf(t[3]))};
            st2c(ov, c.rs, O_H1F + (tile * 14 + cp) * 1024 + ((2 * w + (g >> 1)) * 16 + q) * 16 + (g & 1) * 8, 0);
            TS(seg, 3);
        }
    }
    vm0();
    if (w < 2 && lane == 0) st1c(1u, c.rs, O_FH1 + ((126 + qt) * 32 + 2 * cp + w) * 4, 0);
}

DEV void role_ffn1(const Ctx& c, int f, const u16* __restrict__ Wg, const u16* __restrict__ Wu) {
    const int w = c.w, lane = c.lane, g = c.g, q = c.r;
    const int qt = f & 1, fb = f >> 1, ft = 8 * fb + w;
    int budget = 1 << 16;
    v4u W[28];
    HEAD_WAIT();
#pragma unroll
    for (int ks = 0; ks < 14; ++ks) {
        W[ks] = gatherT(Wg, 1792, 32 * ks + 8 * g, 16 * ft + q);
        W[14 + ks] = gatherT(Wu, 1792, 32 * ks + 8 * g, 16 * ft + q);
    }
    v4u* hn = (v4u*)c.lds;
    f2* st = (f2*)(c.lds + 16384);
    const bool two = w < 6;
    const unsigned fl = O_FH1 + (qt * 32 + 2 * w + (lane & 1) + (two ? (lane & 2) * 8 : 0)) * 4;
    bool pre = false;
    v4u h0, h1 = {0, 0, 0, 0};
    for (int seg = 0; seg < 64; ++seg) {
        const int tile = 2 * seg + qt;
        if (pre && seg >= FASTH && !__all(max(umx(h0), umx(h1)) != SENT)) {
            pre = false;
            vm0();
            if (lane == 0) st1c(1u, c.rs, O_FACT + ((tile - 2) * 128 + ft) * 4, 0);
        }
        if (!pre) {
            if (seg < FASTH) wait_flags(c.rs, fl + seg * 256, true, budget);
            for (;;) {
                MEMBAR();
                h0 = ld4c(c.rs, O_H1F + (tile * 14 + w) * 1024 + lane * 16, 0);
                if (two) h1 = ld4c(c.rs, O_H1F + (tile * 14 + w + 8) * 1024 + lane * 16, 0);
                if (seg < FASTH) break;
                if (__all(max(umx(h0), umx(h1)) != SENT)) break;
                if (--budget < 0) break;
                FSLEEP;
            }
            vm0();
        }
        TS(seg, 0);
        MEMBAR();
        const unsigned nf = ld1c(c.rs, fl + (seg < 63 ? seg + 1 : 63) * 256, 0);
        MEMBAR();
        float s = 0, ss = 0;
#pragma unroll
        for (int i = 0; i < 4; ++i) {
            const float a = lo16(h0[i]), b = hi16(h0[i]), cc = lo16(h1[i]), d = hi16(h1[i]);
            s += (a + b) + (cc + d);
            ss += (a * a + b * b) + (cc * cc + d * d);
        }
        s = sw32add(sw16add(s)); ss = sw32add(sw16add(ss));
        if (lane < 16) st[w * 16 + lane] = (f2){s, ss};
        bar_lds();
        f2 tt = st[q];
#pragma unroll
        for (int i = 1; i < 8; ++i) tt += st[i * 16 + q];
        const float mean = tt[0] * (1.0f / 448.0f), mb = rbf(mean);
        const float var = fmaxf(tt[1] * (1.0f / 448.0f) - mean * mean, 0.0f);
        const float rd = __builtin_amdgcn_rcpf(rbf(__builtin_amdgcn_sqrtf(rbf(rbf(var) + 1e-5f))));
        v4u o0, o1;
#pragma unroll
        for (int i = 0; i < 4; ++i) {
            const unsigned d0 = pk2(lo16(h0[i]) - mb, hi16(h0[i]) - mb), d1 = pk2(lo16(h1[i]) - mb, hi16(h1[i]) - mb);
            o0[i] = pk2(lo16(d0) * rd, hi16(d0) * rd);
            o1[i] = pk2(lo16(d1) * rd, hi16(d1) * rd);
        }
        hn[w * 64 + lane] = o0;
        if (two) hn[(w + 8) * 64 + lane] = o1;
        vm0();
        if (pre && lane == 0) st1c(1u, c.rs, O_FACT + ((tile - 2) * 128 + ft) * 4, 0);
        pre = (__all(nf != 0) || seg + 1 >= FASTH) && seg < 63;
        MEMBAR();
        if (pre) {
            h0 = ld4c(c.rs, O_H1F + ((tile + 2) * 14 + w) * 1024 + lane * 16, 0);
            h1 = ld4c(c.rs, O_H1F + ((tile + 2) * 14 + (two ? w + 8 : w)) * 1024 + lane * 16, 0);
        }
        bar_lds();
        TS(seg, 1);
        v4u b[14];
#pragma unroll
        for (int ks = 0; ks < 14; ++ks) b[ks] = hn[ks * 64 + lane];
        __builtin_amdgcn_sched_barrier(0);
        f4 ga = {0, 0, 0, 0}, ua = ga;
#pragma unroll
        for (int ks = 0; ks < 14; ++ks) {
            ga = mm32(W[ks], b[ks], ga);
            ua = mm32(W[14 + ks], b[ks], ua);
        }
        float av[4];
#pragma unroll
        for (int i = 0; i < 4; ++i) av[i] = fmaxf(rbf(ga[i]), 0.0f) * rbf(ua[i]);
        const v2u ov = {pk2c(av[0], av[1]), pk2c(av[2], av[3])};
        st2c(ov, c.rs, O_ACT + (tile * 56 + (ft >> 1)) * 1024 + ((2 * (ft & 1) + (g >> 1)) * 16 + q) * 16 + (g & 1) * 8, 0);
        TS(seg, 2);
        if (!pre) {
            vm0();
            if (lane == 0) st1c(1u, c.rs, O_FACT + (tile * 128 + ft) * 4, 0);
        } else if (!two) h1 = (v4u){0, 0, 0, 0};
        TS(seg, 3);
    }
}

DEV void role_ffn2(const Ctx& c, int o, const u16* __restrict__ Wd, u16* __restrict__ out) {
    const int w = c.w, lane = c.lane, g = c.g, q = c.r;
    const int cp = o >> 1, qt = o & 1;
    int budget = 1 << 16;
    v4u W[14];
    HEAD_WAIT();
#pragma unroll
    for (int i = 0; i < 7; ++i) {
        W[i] = gatherT(Wd, 448, 32 * (7 * w + i) + 8 * g, 32 * cp + q);
        W[7 + i] = gatherT(Wd, 448, 32 * (7 * w + i) + 8 * g, 32 * cp + 16 + q);
    }
    for (int seg = 0; seg < 64; ++seg) {
        const int tile = 2 * seg + qt;
        if (seg < FASTA) wait_flags(c.rs, O_FACT + (tile * 128 + 14 * w + lane) * 4, lane < 14, budget);
        v4u A[7];
        v2u hr = {0, 0};
        for (;;) {
            MEMBAR();
#pragma unroll
            for (int i = 0; i < 7; ++i) A[i] = ld4c(c.rs, O_ACT + (tile * 56 + 7 * w + i) * 1024 + lane * 16, 0);
            if (w < 2) hr = ld2c(c.rs, O_H1F + (tile * 14 + cp) * 1024 + ((2 * w + (g >> 1)) * 16 + q) * 16 + (g & 1) * 8, 0);
            if (seg < FASTA) break;
            unsigned mx = umx2(hr);
#pragma unroll
            for (int i = 0; i < 7; ++i) mx = max(mx, umx(A[i]));
            if (__all(mx != SENT)) break;
            if (--budget < 0) break;
            FSLEEP;
        }
        TS(seg, 0);
        f4 a0 = {0, 0, 0, 0}, a1 = a0;
#pragma unroll
        for (int i = 0; i < 7; ++i) { a0 = mm32(W[i], A[i], a0); a1 = mm32(W[7 + i], A[i], a1); }
        unsigned char* red = c.lds + (seg & 1) * 16384;
        *(f4*)(red + w * 2048 + lane * 16) = a0;
        *(f4*)(red + w * 2048 + 1024 + lane * 16) = a1;
        __syncthreads();
        if (w < 2) {
            const f4 t = red8(red + w * 1024 + lane * 16);
            const v2u ov = {pk2(lo16(hr.x) + rbf(t[0]), hi16(hr.x) + rbf(t[1])), pk2(lo16(hr.y) + rbf(t[2]), hi16(hr.y) + rbf(t[3]))};
            *(v2u*)(out + (size_t)(tile * 16 + q) * 448 + 32 * cp + 16 * w + 4 * g) = ov;
            TS(seg, 3);
        }
    }
}

__global__ void __launch_bounds__(512, 2) k_main(const u16* __restrict__ x, const u16* __restrict__ Wg, const u16* __restrict__ Wu,
                                                 const u16* __restrict__ Wd, u16* __restrict__ out, unsigned char* __restrict__ ws) {
    __shared__ __attribute__((aligned(16))) unsigned char lds[96 * 1024];
    Ctx c;
    c.rs = __builtin_amdgcn_make_buffer_rsrc(ws, 0, WS_SPAN, 0x00020000);
    c.lds = lds;
    c.tid = threadIdx.x; c.lane = c.tid & 63; c.w = __builtin_amdgcn_readfirstlane(c.tid >> 6); c.g = c.lane >> 4; c.r = c.lane & 15;
    int* slot = (int*)(lds + 65536 + 1024);
    if (c.tid == 0) {
        unsigned xcc;
        asm volatile("s_getreg_b32 %0, hwreg(HW_REG_XCC_ID)" : "=s"(xcc));
        int res = -1;
        for (int i = 0; i < 8 && res < 0; ++i) {
            const unsigned xx = (xcc + i) & 7u;
            const unsigned t = __hip_atomic_fetch_add((unsigned*)(ws + O_TICK + xx * 128), 1u, __ATOMIC_RELAXED, __HIP_MEMORY_SCOPE_AGENT);
            if (t < 32) res = (int)(xx * 32 + t);
        }
        *slot = res;
    }
    __syncthreads();
    const int sl = __builtin_amdgcn_readfirstlane(*slot);
    if (sl < 0) return;
    const int xx = sl >> 5, t = sl & 31;
    if (xx < 4) {
        if (t < 16) role_score(c, xx * 16 + t);
        else if (t < 23) role_out(c, xx * 7 + t - 16);
        else if (t < 30) role_ffn1(c, xx * 7 + t - 23, Wg, Wu);
        else role_tok(c, xx * 2 + t - 30);
    }
    else {
        const int k = (xx - 4) + 4 * t;
        if (k < 28) role_kv<true>(c, k);
        else if (k < 56) role_kv<false>(c, k);
        else if (k < 72) role_atts(c, k - 56);
        else if (k < 100) role_atto(c, k - 72, x);
        else role_ffn2(c, k - 100, Wd, out);
    }
}

extern "C" void kernel_launch(void* const* d_in, const int* in_sizes, int n_in, void* d_out, int out_size, void* d_ws, size_t ws_size,
                              hipStream_t stream) {
    unsigned char* ws = (unsigned char*)(((size_t)d_ws + (2 * MB - 1)) & ~(size_t)(2 * MB - 1));
    k_prep<<<256, 512, 0, stream>>>((const u16*)d_in[0], (const u16*)d_in[1], (const u16*)d_in[2], (const u16*)d_in[3], (const u16*)d_in[4],
                                    (const u16*)d_in[5], ws);
    k_main<<<256, 512, 0, stream>>>((const u16*)d_in[0], (const u16*)d_in[8], (const u16*)d_in[9], (const u16*)d_in[10], (u16*)d_out, ws);
}
```
